```python
import torch

def _softmax(x, axis=-1):
    return torch.softmax(x, dim=axis)
def _gelu(x):
    return torch.nn.functional.gelu(x, approximate="tanh")
def _silu(x):
    return torch.nn.functional.silu(x)
def _sigmoid(x):
    return torch.sigmoid(x)
def _layernorm(x, axis=-1, eps=1e-5):
    m = x.mean(dim=axis, keepdim=True)
    v = x.var(dim=axis, keepdim=True, unbiased=False)
    return (x - m) / torch.sqrt(v + eps)
def _scatter_add(tgt, idx, src, axis=0):
    return tgt.clone().index_add_(axis, idx, src)
def _segment_sum(src, seg, n):
    out = torch.zeros((n,) + src.shape[1:], dtype=src.dtype)
    return out.index_add_(0, seg, src)
def _pad(x, pw):
    flat = tuple(v for pair in reversed(pw) for v in pair)
    return torch.nn.functional.pad(x, flat)

kernel_name = 'gated_mix__linear_attn_block__moe_arch_mod__prompt'

def setup_inputs():

    _w = (lambda a: (lambda t: t.to(torch.bfloat16) if t.is_floating_point() else t)(torch.as_tensor(a)))
    def _A_setup():
        B, S, D = 1, 2048, 1024
        return {
            "x": _w((lambda *s: torch.randn(*s, dtype=torch.bfloat16) * 0.05)(B, S, D)),
            "wqkv": _w((lambda *s: torch.randn(*s, dtype=torch.bfloat16) * 0.8)(D, 3 * D)),
            "wo": _w((lambda *s: torch.randn(*s, dtype=torch.bfloat16) * 0.1)(D, D)),
        }
    _A_in = _A_setup()

    def _B_setup():
        B, S, D, F, C = 1, 2048, 1024, 3584, 1
        return {
            "x": _w((lambda *s: torch.randn(*s, dtype=torch.bfloat16) * 0.05)(B, S, D)),
            "w_r": _w((lambda *s: torch.randn(*s, dtype=torch.bfloat16) * 1.6)(D)),
            "w_gate": _w((lambda *s: torch.randn(*s, dtype=torch.bfloat16) * 0.1)(D, F)),
            "w_up": _w((lambda *s: torch.randn(*s, dtype=torch.bfloat16) * 0.1)(D, F)),
            "w_down": _w((lambda *s: torch.randn(*s, dtype=torch.bfloat16) * 0.1)(F, D)),
        }
    _B_in = _B_setup()

    return {
        "A_x": _A_in["x"],
        "A_wqkv": _A_in["wqkv"],
        "A_wo": _A_in["wo"],
        "B_x": _B_in["x"],
        "B_w_r": _B_in["w_r"],
        "B_w_gate": _B_in["w_gate"],
        "B_w_up": _B_in["w_up"],
        "B_w_down": _B_in["w_down"],
        "gate_w": _w((lambda *s: torch.randn(*s, dtype=torch.bfloat16) * 0.05)(1024) * 16.0),
    }

def reference(A_x, A_wqkv, A_wo, B_x, B_w_r, B_w_gate, B_w_up, B_w_down, gate_w):
    def _A_ref(x, wqkv, wo):
        B, S, D = x.shape
        H = 32; Dh = D // H
        qkv = (lambda x, *d: x.permute(*d))((x @ wqkv).reshape(B, S, 3, H, Dh), 2, 0, 3, 1, 4)
        q, k, v = qkv[0], qkv[1], qkv[2]
        phi = lambda t: torch.where(t > 0, t + 1, torch.exp(t)) + 1e-6
        pq, pk = phi(q), phi(k)
        kv = torch.einsum("bhsd,bhse->bhde", pk, v)
        z = torch.einsum("bhsd->bhd", pk)
        num = torch.einsum("bhqd,bhde->bhqe", pq, kv)
        den = torch.einsum("bhqd,bhd->bhq", pq, z)[..., None] + 1e-6
        o = num / den
        return (lambda x, *d: x.permute(*d))(o, 0, 2, 1, 3).reshape(B, S, D) @ wo

    def _B_ref(x, w_r, w_gate, w_up, w_down):
        B, S, D = x.shape
        C = 1
        r = x @ w_r
        idx = torch.argsort(-r, axis=-1)[:, :C]
        idx_e = (lambda x, n, axis: torch.repeat_interleave(x, n, dim=axis))(idx.reshape(B, C, 1), D, axis=2)
        x_sel = (lambda x, i, axis: torch.take_along_dim(x, i, dim=axis))(x, idx_e, axis=1)
        g = _silu(x_sel @ w_gate)
        u = x_sel @ w_up
        y = (g * u) @ w_down
        r_sel = _sigmoid((lambda x, i, axis: torch.take_along_dim(x, i, dim=axis))(r, idx, axis=1))
        y = y * r_sel.reshape(B, C, 1)
        b_off = (torch.arange(B) * S).reshape(B, 1)
        flat_idx = (idx + b_off).reshape(B * C)
        zero = (x * 0.0).reshape(B * S, D)
        upd = _scatter_add(zero, flat_idx, y.reshape(B * C, D), axis=0)
        return x + upd.reshape(B, S, D)

    x = A_x
    g = _sigmoid((x * gate_w).sum(-1, keepdims=True))
    a = _A_ref(x, A_wqkv, A_wo)
    b = _B_ref(x, B_w_r, B_w_gate, B_w_up, B_w_down)
    return g * a + (1 - g) * b
```

```cpp
#include <hip/hip_runtime.h>
#include <hip/hip_bf16.h>
#include <stdint.h>

typedef unsigned short u16;
typedef unsigned long long u64;
typedef __attribute__((ext_vector_type(8))) short bf16x8;
typedef __attribute__((ext_vector_type(4))) short s16x4;
typedef __attribute__((ext_vector_type(4))) float f32x4;
typedef __attribute__((ext_vector_type(2))) float f32x2;
typedef __attribute__((ext_vector_type(2))) __bf16 bf16x2_t;
typedef __attribute__((ext_vector_type(2))) _Float16 f16x2_t;
typedef __attribute__((ext_vector_type(8))) _Float16 f16x8_t;
typedef __attribute__((ext_vector_type(4))) unsigned int u32x4;
typedef __attribute__((ext_vector_type(2))) unsigned int u32x2;
typedef __attribute__((address_space(3))) s16x4* lds_s16x4_ptr;
typedef __attribute__((address_space(1))) unsigned gu32;
typedef __attribute__((address_space(1))) u64 gu64;
typedef __attribute__((address_space(3))) unsigned lu32;
typedef __attribute__((address_space(3))) u32x2 lu32x2;
typedef __attribute__((address_space(3))) u32x4 lu32x4;
typedef __attribute__((address_space(3))) f32x4 lf32x4;
#define RLX_AGENT __ATOMIC_RELAXED, __HIP_MEMORY_SCOPE_AGENT
#define RLX_WG __ATOMIC_RELAXED, __HIP_MEMORY_SCOPE_WORKGROUP

#if defined(__HIP_DEVICE_COMPILE__)
#define NOPK __attribute__((target("no-packed-fp32-ops")))
#else
#define NOPK
#endif

#define SEQ 2048
#define DM 1024
#define N1 3072
#define FF 3584
#define NH 32

#define OFF_EPOCH  ((size_t)0)
#define OFF_RG     ((size_t)4096)
#define OFF_GG     ((size_t)16384)
#define OFF_GU     ((size_t)262144)
#define OFF_YG     ((size_t)524288)
#define OFF_KVP    ((size_t)1 << 20)
#define OFF_ZP     ((size_t)2 << 20)
#define OFF_KVFLAG ((size_t)3 << 20)
#define OFF_OFLAG  (((size_t)3 << 20) + 65536)
#define OFF_OF     ((size_t)4 << 20)

#define SPIN_CAP (1u << 18)

__device__ __forceinline__ float u2f(unsigned u) { return __builtin_bit_cast(float, u); }
__device__ __forceinline__ unsigned f2u(float f) { return __builtin_bit_cast(unsigned, f); }
__device__ __forceinline__ bool all_true(bool ok) { return __builtin_amdgcn_ballot_w64(!ok) == 0ull; }
__device__ __forceinline__ float shfl_f(float v, int src) { return u2f((unsigned)__builtin_amdgcn_ds_bpermute(src << 2, (int)f2u(v))); }
__device__ __forceinline__ unsigned cvt_pk(float a, float b) {
  f32x2 v = {a, b};
  return __builtin_bit_cast(unsigned, __builtin_convertvector(v, bf16x2_t));
}
__device__ __forceinline__ float blo(unsigned u) { return u2f(u << 16); }
__device__ __forceinline__ float bhi(unsigned u) { return u2f(u & 0xFFFF0000u); }
__device__ __forceinline__ float bfr(float f) { return blo(cvt_pk(f, f)); }
__device__ __forceinline__ float phi_e(float u) {
  return __builtin_amdgcn_fmed3f(__builtin_amdgcn_exp2f(__builtin_fmaf(u, 1.44269504088896341f, -1.44269504088896341f)), 0.f, 1.f);
}
__device__ __forceinline__ float fmax_raw(float a, float b) { return __builtin_elementwise_maximum(a, b); }
__device__ __forceinline__ unsigned phi2a(float a, float b) { return cvt_pk(fmax_raw(a, phi_e(a)), fmax_raw(b, phi_e(b))); }
__device__ __forceinline__ unsigned eps2(unsigned p) { return cvt_pk(blo(p) + 1e-6f, bhi(p) + 1e-6f); }
typedef __attribute__((ext_vector_type(2))) unsigned short u16x2;
__device__ __forceinline__ unsigned pkmin(unsigned a, unsigned b) {
  return __builtin_bit_cast(unsigned, __builtin_elementwise_min(__builtin_bit_cast(u16x2, a), __builtin_bit_cast(u16x2, b)));
}
template <int N>
__device__ __forceinline__ void phi_fix(unsigned (&p)[N], unsigned m2) {
  const unsigned m = min(m2 & 0xFFFFu, m2 >> 16);
  if (__builtin_expect(__builtin_amdgcn_ballot_w64(m < 0x3A80u) != 0ull, 0)) {
#pragma unroll
    for (int i = 0; i < N; ++i) p[i] = eps2(p[i]);
  }
}
__device__ __forceinline__ float sigmoid_f(float x) { return 1.0f / (1.0f + __expf(-x)); }

template <int CTRL, int RM = 0xf> __device__ __forceinline__ int dppi(int old, int v) {
  return __builtin_amdgcn_update_dpp(old, v, CTRL, RM, 0xf, false);
}
template <int CTRL, int RM = 0xf> __device__ __forceinline__ float dppf(float v) {
  return __builtin_bit_cast(float, dppi<CTRL, RM>(0, __builtin_bit_cast(int, v)));
}
__device__ __forceinline__ unsigned wave_max(unsigned v) {
  v = max(v, (unsigned)dppi<0x111>(0, (int)v)); v = max(v, (unsigned)dppi<0x112>(0, (int)v));
  v = max(v, (unsigned)dppi<0x114>(0, (int)v)); v = max(v, (unsigned)dppi<0x118>(0, (int)v));
  v = max(v, (unsigned)dppi<0x142, 0xa>(0, (int)v)); v = max(v, (unsigned)dppi<0x143, 0xc>(0, (int)v));
  return (unsigned)__builtin_amdgcn_readlane((int)v, 63);
}
__device__ __forceinline__ float sum_x16(float v) {
  const unsigned u = f2u(v);
  const auto r = __builtin_amdgcn_permlane16_swap(u, u, false, false);
  return u2f(r[0]) + u2f(r[1]);
}
__device__ __forceinline__ float sum_x32(float v) {
  const unsigned u = f2u(v);
  const auto r = __builtin_amdgcn_permlane32_swap(u, u, false, false);
  return u2f(r[0]) + u2f(r[1]);
}
__device__ __forceinline__ float merge32(float a, float b) {
  const auto r = __builtin_amdgcn_permlane32_swap(f2u(a), f2u(b), false, false);
  return u2f(r[0]) + u2f(r[1]);
}
__device__ __forceinline__ float merge16(float a, float b) {
  const auto r = __builtin_amdgcn_permlane16_swap(f2u(a), f2u(b), false, false);
  return u2f(r[0]) + u2f(r[1]);
}
__device__ __forceinline__ float sum_hi3(float v) {
  v += dppf<0x128>(v);
  return sum_x32(sum_x16(v));
}
typedef __attribute__((address_space(3))) void* ldsp;
__device__ __forceinline__ void bufl16(__amdgpu_buffer_rsrc_t r, char* l, unsigned voff, unsigned soff) {
#if defined(__HIP_DEVICE_COMPILE__)
  __builtin_amdgcn_raw_ptr_buffer_load_lds(r, (ldsp)l, 16, voff, soff, 0, 0);
#endif
}
#define MKRSRC(p, n) __builtin_amdgcn_make_buffer_rsrc((void*)(p), 0, (n), 0x00020000)
#define WAIT_VM(n) asm volatile("s_waitcnt vmcnt(%0)" ::"n"(n) : "memory")
#define WAIT_LGKM0() asm volatile("s_waitcnt lgkmcnt(0)" ::: "memory")
#define BAR() do { __builtin_amdgcn_sched_barrier(0); asm volatile("s_barrier" ::: "memory"); __builtin_amdgcn_sched_barrier(0); } while (0)
#define CFENCE() asm volatile("" ::: "memory")
#define MFMA(a, b, c) __builtin_amdgcn_mfma_f32_16x16x32_bf16((a), (b), (c), 0, 0, 0)
#define PIN(v) asm volatile("" : "+v"(v))
#define AMFMA(acc, a, b) asm volatile("v_mfma_f32_16x16x32_bf16 %0, %1, %2, %0" : "+v"(acc) : "v"(a), "v"(b))
#define NOP8(t) asm volatile("s_nop 7" : "+v"(t[0][0]), "+v"(t[0][1]), "+v"(t[1][0]), "+v"(t[1][1]), "+v"(t[2][0]), "+v"(t[2][1]), "+v"(t[3][0]), "+v"(t[3][1]))
#define SB() __builtin_amdgcn_sched_barrier(0)
#define TRD(p) ((s16x4)__builtin_amdgcn_ds_read_tr16_b64_v4i16((lds_s16x4_ptr)(p)))
#define CAT(lo, hi) __builtin_shufflevector(lo, hi, 0, 1, 2, 3, 4, 5, 6, 7)
#define FRAG(x) __builtin_bit_cast(bf16x8, x)

#define PACE   57
#define BURST  30
#define R_AT   2
#define R_GAP  2
#define DN_AT  4
#define WO_E   4
#define AGZ    8
#define OPF    3

__device__ __forceinline__ unsigned rfl(unsigned v) { return (unsigned)__builtin_amdgcn_readfirstlane((int)v); }
template <int SLEEP>
__device__ __forceinline__ unsigned wait_ge(volatile lu32* p, unsigned need) {
  unsigned v = 0;
#pragma unroll 1
  for (unsigned spins = 0; spins < (1u << 22); ++spins) {
    v = rfl(*p);
    if (v >= need) break;
    __builtin_amdgcn_s_sleep(SLEEP);
  }
  CFENCE();
  return v;
}

#define SLOT     40960
#define L_X1     147456
#define L_X2     155904
#define L_RED    160128
#define L_CTL    161408
#define L_TOTAL  161664
#define WOPOS(c) ((c) < 3 ? 13 + (c) : (c) < 8 ? (c) : (c) < 11 ? (c) - 8 : (c) - 3)
#define C_F15  0
#define C_WO   2
#define C_AG   4
#define C_TAG  8
#define C_IDX  10
#define C_X1   12
#define C_KARR 16
#define C_OARR 17
#define C_ORDY 18
#define C_GU   19
#define C_DN   20
#define C_T6   21
#define C_Y    23
#define C_X2   24
#define C_XZ   32

NOPK __global__ __launch_bounds__(512, 2) void fused_kernel(const u16* __restrict__ x, const u16* __restrict__ wqkv,
                                                            const u16* __restrict__ wo, char* ws, u16* __restrict__ out,
                                                            const u16* __restrict__ w_r, const u16* __restrict__ gate_w,
                                                            const u16* __restrict__ w_gate, const u16* __restrict__ w_up,
                                                            const u16* __restrict__ w_down) {
  extern __shared__ __attribute__((aligned(16))) char smem[];
  const int tid = threadIdx.x, lane = tid & 63;
  const int w = __builtin_amdgcn_readfirstlane(tid >> 6);
  asm volatile("" ::"s"(x), "s"(wqkv), "s"(wo));
  const int bid = blockIdx.x, xcd = bid & 7, ix = bid >> 3;
  const int ml = ix >> 2, nl = ix & 3;
  const int mt = (xcd >> 2) * 8 + ml, nt = (xcd & 3) * 4 + nl;
  const int m0 = mt * 128;
  const int mt3 = mt, nt3 = nt ^ 1;
  const bool slow = (int)((((uintptr_t)wqkv >> 7) + nt) & 7) == 3;
  const int koff = (4 * ml - (slow ? 2 * (ml >> 2) : 0) + 4 * (xcd & 3)) & 15;
  const int rot = 4 * (xcd & 3);
  volatile lu32* misc = (volatile lu32*)(smem + L_CTL);

  if (__builtin_expect((w >> 1) == 2, 1)) {
    const int p = w - 4;
    const __amdgpu_buffer_rsrc_t ra = MKRSRC(x, SEQ * DM * 2), rb = MKRSRC(wqkv, DM * N1 * 2), rw = MKRSRC(wo, DM * DM * 2);
    const int row = 8 * p + (lane >> 3);
    const unsigned va = (unsigned)((m0 + row) * (DM * 2) + (((lane & 7) ^ ((row >> 1) & 7)) * 16));
    const unsigned vb = (unsigned)(row * (N1 * 2) + 128 * nt + (((lane & 7) ^ (((lane >> 3) & 2) | (p << 2))) * 16));
#define ISSUE(lb, ktr)                                                                                     \
  do {                                                                                                     \
    _Pragma("unroll") for (int i = 0; i < 16; i += 2) bufl16(ra, (lb) + i * 1024, va, (ktr) * 128 + i * (8 * DM * 2)); \
    _Pragma("unroll") for (int i = 0; i < 24; i += 2) bufl16(rb, (lb) + 16384 + i * 1024, vb, (ktr) * (64 * N1 * 2) + (i & 7) * (8 * N1 * 2) + (i >> 3) * (DM * 2)); \
  } while (0)
#define PACED_BAR()                                                                                        \
  do {                                                                                                     \
    if (p == 0) {                                                                                          \
      unsigned now;                                                                                        \
      do { now = (unsigned)__builtin_amdgcn_s_memrealtime(); } while ((int)(now - tp) < 0);                \
      tp = ((int)(now - tp) > BURST ? now - BURST : tp) + PACE;                                            \
    }                                                                                                      \
    BAR();                                                                                                 \
  } while (0)
    char* l0 = smem + p * 1024;
    ISSUE(l0, koff); ISSUE(l0 + SLOT, (koff + 1) & 15);
    WAIT_VM(20);
    BAR();
    ISSUE(l0 + 2 * SLOT, (koff + 2) & 15);
    int set = 0;
    unsigned tp = (unsigned)__builtin_amdgcn_s_memrealtime() - PACE;
#pragma unroll 1
    for (int t = 0; t < 13; ++t) {
      WAIT_VM(20);
      PACED_BAR();
      char* lb = l0 + set * SLOT;
      const int ktr = (koff + t + 3) & 15;
      ISSUE(lb, ktr);
      set = set == 2 ? 0 : set + 1;
    }
    const unsigned vw = (unsigned)(row * (DM * 2) + 128 * nt3 + (((lane & 7) ^ ((lane >> 3) & 6)) * 16));
#define WO_CHUNK(c) _Pragma("unroll") for (int i = 0; i < 8; i += 2) bufl16(rw, l0 + 16384 + WOPOS(c) * 8192 + i * 1024, vw, ((rot + (c)) & 15) * (64 * DM * 2) + i * (8 * DM * 2))
    WO_CHUNK(0); WO_CHUNK(1); WO_CHUNK(2);
    WAIT_VM(32);
    PACED_BAR();
    WAIT_VM(12);
    if (lane == 0) misc[C_F15 + p] = 1u;
#pragma unroll 1
    for (int c = 3; c < WO_E; ++c) WO_CHUNK(c);
#pragma unroll 1
    for (unsigned spins = 0; spins < (1u << 22) && !all_true(misc[C_AG + (lane & 3)] != 0u); ++spins) __builtin_amdgcn_s_sleep(2);
    CFENCE();
#pragma unroll 1
    for (int c = WO_E; c < 16; ++c) { WO_CHUNK(c); WAIT_VM(28); }
    WAIT_VM(0);
    if (lane == 0) misc[C_WO + p] = 1u;
#undef ISSUE
#undef PACED_BAR
    if (p == 0) return;
    const unsigned tag = rfl(misc[C_TAG]);
    gu32* of = (gu32*)(ws + OFF_OFLAG + (mt3 * 16 + ((rot + lane) & 15)) * 128);
    const unsigned t6 = wait_ge<1>(misc + C_T6, 1u);
    while ((unsigned)__builtin_amdgcn_s_memrealtime() - t6 < 25u) __builtin_amdgcn_s_sleep(1);
#pragma unroll 1
    for (unsigned spins = 0;; ++spins) {
      const unsigned f = __hip_atomic_load(of, RLX_AGENT);
      const unsigned miss = (unsigned)__builtin_amdgcn_ballot_w64(f != tag) & 0xFFFFu;
      const unsigned m = (miss == 0u || spins > SPIN_CAP) ? 16u : (unsigned)__builtin_ctz(miss);
      if (lane == 0) misc[C_ORDY] = m;
      if (m == 16u) break;
      __builtin_amdgcn_s_sleep(2);
    }
    return;
  }

  if (__builtin_expect(w >= 6, 0)) {
    const int pa = w - 6;
    float* red = (float*)(smem + L_RED);
    const bool help = bid >= 224, gemv = bid < 252;
    const int gmat = bid & 1;
    const u16* wgu = gmat ? w_up : w_gate;
    const int sres = (int)(3u - (unsigned)((uintptr_t)wgu >> 7)) & 7;
    const int gfs = help ? sres + 8 * (((bid - 224) >> 1) % 7) : (bid >> 1) % 56;
    const int gdh = (help ? ((bid - 224) >> 1) / 7 : (bid >> 1) / 56) & 1;
    const bool act0 = !help, act1 = gemv && (help || (gfs & 7) != sres);
    const int gd0 = gdh * 512 + 256 * pa + 32 * (lane >> 3);
    const __amdgpu_buffer_rsrc_t rgu0 = MKRSRC(wgu, act0 ? DM * FF * 2 : 0), rgu1 = MKRSRC(wgu, act1 ? DM * FF * 2 : 0);
    const unsigned wpo = (unsigned)((gd0 * FF + gfs * 64 + 8 * (lane & 7)) * 2);
    u32x4 wv[32], wd[14];
    const int fg = bid >> 4, cg = bid & 15;
    const int f0 = fg * 224 + pa * 112;
    const __amdgpu_buffer_rsrc_t rdn = MKRSRC(w_down, DM * FF * 2);
    const unsigned wqo = (unsigned)(((f0 + (lane >> 3)) * DM + cg * 64 + 8 * (lane & 7)) * 2);
    asm volatile("" ::"s"(w_r), "s"(gate_w), "s"(ws), "s"(w_gate), "s"(w_up), "s"(w_down));
    const int tb = DN_AT + (ix & 7);
    unsigned pf0, pf1;
#define COLD_LOADS(k)                                                                                      \
  do {                                                                                                     \
    if (tb == (k)) {                                                                                       \
      _Pragma("unroll") for (int i = 0; i < 14; ++i) wd[i] = __builtin_amdgcn_raw_buffer_load_b128(rdn, wqo, i * (8 * DM * 2), 2); \
      const u16* pp_ = wo + (size_t)(128 * ml + 64 * pa + (lane >> 1)) * DM + 64 * nt3 + (lane & 1) * 32;  \
      pf0 = *(const unsigned*)pp_; pf1 = *(const unsigned*)(pp_ + 32 * DM);                                \
      SB();                                                                                                \
    }                                                                                                      \
  } while (0)
    {
#pragma unroll
      for (int t = 0; t <= R_AT; ++t) BAR();
      const int r0 = m0 + nt * 8 + 4 * pa;
      u32x4 xr[4][2], rv[2], gv[2];
#pragma unroll
      for (int i = 0; i < 2; ++i) {
        rv[i] = *(const u32x4*)(w_r + i * 512 + lane * 8);
        gv[i] = *(const u32x4*)(gate_w + i * 512 + lane * 8);
      }
#pragma unroll
      for (int r = 0; r < 4; ++r)
#pragma unroll
        for (int i = 0; i < 2; ++i) xr[r][i] = *(const u32x4*)(x + (size_t)(r0 + r) * DM + i * 512 + lane * 8);
      const unsigned ep = __hip_atomic_load((gu32*)(ws + OFF_EPOCH), RLX_AGENT);
      {
        const u32x4 nw = {~0u, ~0u, ~0u, ~0u};
        const __amdgpu_buffer_rsrc_t kvr = MKRSRC(ws + OFF_KVP, NH * 2 * 16 * 1024), zr = MKRSRC(ws + OFF_ZP, NH * 2 * 16 * 128);
        const unsigned rec = (unsigned)((2 * nt + pa) * 32 + mt);
        __builtin_amdgcn_raw_buffer_store_b128(nw, kvr, rec * 1024 + lane * 16, 0, 16);
        __builtin_amdgcn_raw_buffer_store_b128(nw, kvr, rec * 1024 + lane * 16, 16384, 16);
        if (lane < 16) __builtin_amdgcn_raw_buffer_store_b128(nw, zr, (rec + (lane >> 3) * 16) * 128 + (lane & 7) * 16, 0, 16);
      }
#pragma unroll
      for (int t = R_AT + 1; t < R_AT + R_GAP; ++t) BAR();
      float racc[4], gacc[4];
#pragma unroll
      for (int r = 0; r < 4; ++r) {
        BAR();
        if (r == 0) __builtin_amdgcn_s_waitcnt(0x0F72);
        racc[r] = 0.f; gacc[r] = 0.f;
#pragma unroll
        for (int i = 0; i < 2; ++i)
#pragma unroll
          for (int e = 0; e < 4; ++e) {
            const float x0 = blo(xr[r][i][e]), x1 = bhi(xr[r][i][e]);
            racc[r] += x0 * blo(rv[i][e]); racc[r] += x1 * bhi(rv[i][e]);
            const unsigned pr = cvt_pk(x0 * blo(gv[i][e]), x1 * bhi(gv[i][e]));
            gacc[r] += blo(pr); gacc[r] += bhi(pr);
          }
        PIN(racc[r]); PIN(gacc[r]);
        SB();
        COLD_LOADS(R_AT + R_GAP + r);
      }
      BAR();
      COLD_LOADS(R_AT + R_GAP + 4);
      float rs = merge16(merge32(racc[0], racc[1]), merge32(racc[2], racc[3]));
      float gs = merge16(merge32(gacc[0], gacc[1]), merge32(gacc[2], gacc[3]));
      rs += dppf<0x128>(rs); gs += dppf<0x128>(gs); rs += dppf<0x124>(rs); gs += dppf<0x124>(gs);
      rs += dppf<0x122>(rs); gs += dppf<0x122>(gs); rs += dppf<0x121>(rs); gs += dppf<0x121>(gs);
      const int rr = r0 + ((lane >> 5) | ((lane >> 3) & 2));
      const unsigned rb = cvt_pk(rs, rs) & 0xFFFFu;
      unsigned key = (((rb & 0x8000u) ? (~rb & 0xFFFFu) : (rb | 0x8000u)) << 16) | (0xFFFFu - (unsigned)rr);
      { const auto t_ = __builtin_amdgcn_permlane16_swap(key, key, false, false); key = max(t_[0], t_[1]); }
      { const auto t_ = __builtin_amdgcn_permlane32_swap(key, key, false, false); key = max(t_[0], t_[1]); }
      const unsigned tg = rfl(ep) + 1u;
      if (lane == 0) misc[C_TAG + pa] = pa ? key : tg;
      if (tb <= R_AT + R_GAP + 4) WAIT_VM(14); else WAIT_VM(0);
      WAIT_LGKM0();
      BAR();
      if (pa == 0 && lane == 0) __hip_atomic_store((gu64*)(ws + OFF_RG) + bid, ((u64)tg << 32) | max(key, (unsigned)misc[C_TAG + 1]), RLX_AGENT);
      if ((lane & 15) == 0) __hip_atomic_store((gu64*)(ws + OFF_GG) + rr, ((u64)tg << 32) | f2u(bfr(sigmoid_f(bfr(gs)))), RLX_AGENT);
      COLD_LOADS(R_AT + R_GAP + 5);
#pragma unroll
      for (int t = R_AT + R_GAP + 6; t < 13; ++t) { BAR(); if (t < 12) COLD_LOADS(t); }
    }
    u64 rg[4];
#pragma unroll
    for (int i = 0; i < 4; ++i) rg[i] = __hip_atomic_load((gu64*)(ws + OFF_RG) + i * 64 + lane, RLX_AGENT);
    BAR();
    const unsigned tag = rfl(misc[C_TAG]);
    unsigned idx;
    unsigned bestk;
    bool late;
    {
      unsigned best = 0;
      bool ok = true;
#pragma unroll
      for (int i = 0; i < 4; ++i) { ok = ok && (unsigned)(rg[i] >> 32) == tag; best = max(best, (unsigned)rg[i]); }
      late = !all_true(ok);
      asm volatile("" ::"v"(pf0), "v"(pf1));
      if (__builtin_expect(late, 0)) {
        BAR();
#pragma unroll 1
        for (unsigned spins = 0;; ++spins) {
          u64 rh[4];
#pragma unroll
          for (int i = 0; i < 4; ++i) rh[i] = __hip_atomic_load((gu64*)(ws + OFF_RG) + i * 64 + lane, RLX_AGENT);
          ok = true; best = 0;
#pragma unroll
          for (int i = 0; i < 4; ++i) { ok = ok && (unsigned)(rh[i] >> 32) == tag; best = max(best, (unsigned)rh[i]); }
          if (all_true(ok) || spins > SPIN_CAP) break;
          __builtin_amdgcn_s_sleep(8);
        }
      }
      best = wave_max(best);
      idx = (0xFFFFu - (best & 0xFFFFu)) & (SEQ - 1);
      bestk = best;
    }
    u32x4 xs[4];
#pragma unroll
    for (int i = 0; i < 4; ++i) xs[i] = *(const u32x4*)(x + (size_t)idx * DM + gd0 + 8 * i);
    if (!late) BAR();
#pragma unroll
    for (int i = 0; i < 32; ++i) wv[i] = __builtin_amdgcn_raw_buffer_load_b128(i < 16 ? rgu0 : rgu1, wpo, i * (FF * 2), 2);
    SB();
    if (pa == 0 && lane == 0) misc[C_IDX] = idx + 1u;
    if (bid == 0 && pa == 0 && lane == 0) __hip_atomic_store((gu32*)(ws + OFF_EPOCH), tag, RLX_AGENT);
    if (gemv) {
      float a[8];
#pragma unroll
      for (int e = 0; e < 8; ++e) a[e] = 0.f;
#pragma unroll
      for (int hf = 0; hf < 2; ++hf)
        if (hf ? act1 : act0) {
#pragma unroll
          for (int i = 16 * hf; i < 16 * hf + 16; ++i) {
            const unsigned xw = xs[i >> 3][(i >> 1) & 3];
            const float xv = (i & 1) ? bhi(xw) : blo(xw);
#pragma unroll
            for (int e = 0; e < 4; ++e) { a[2 * e] += xv * blo(wv[i][e]); a[2 * e + 1] += xv * bhi(wv[i][e]); }
            SB();
          }
        }
#pragma unroll
      for (int e = 0; e < 8; ++e) a[e] = sum_hi3(a[e]);
      if (lane < 8) {
        *(f32x4*)(red + pa * 64 + lane * 8) = (f32x4){a[0], a[1], a[2], a[3]};
        *(f32x4*)(red + pa * 64 + lane * 8 + 4) = (f32x4){a[4], a[5], a[6], a[7]};
      }
      CFENCE();
      if (pa == 1) { if (lane == 0) misc[C_GU] = 1u; }
      else {
        (void)wait_ge<1>(misc + C_GU, 1u);
        const float v = red[lane] + red[64 + lane];
        __hip_atomic_store((gu64*)(ws + OFF_GU) + ((help ? 4 : 0) + gdh * 2 + gmat) * FF + gfs * 64 + lane, ((u64)tag << 32) | f2u(v), RLX_AGENT);
      }
    }
    {
      (void)wait_ge<4>(misc + C_AG, 1u);
      __builtin_amdgcn_s_sleep(16);
      float hv[2];
      const int fl = f0 + (lane < 56 ? lane : 55);
      const gu64* gp = (const gu64*)(ws + OFF_GU) + fl;
      bool ex[2][2];
#pragma unroll
      for (int j = 0; j < 2; ++j) {
        ex[j][0] = ((((unsigned)((uintptr_t)w_gate >> 7) + ((fl + 56 * j) >> 6)) & 7u) == 3u);
        ex[j][1] = ((((unsigned)((uintptr_t)w_up >> 7) + ((fl + 56 * j) >> 6)) & 7u) == 3u);
      }
#pragma unroll 1
      for (unsigned spins = 0;; ++spins) {
        u64 g[2][8];
#pragma unroll
        for (int j = 0; j < 2; ++j)
#pragma unroll
          for (int i = 0; i < 8; ++i) {
            g[j][i] = (u64)tag << 32;
            if (i < 4 || ex[j][i & 1]) g[j][i] = __hip_atomic_load(gp + i * FF + j * 56, RLX_AGENT);
          }
        bool ok = true;
#pragma unroll
        for (int j = 0; j < 2; ++j) {
#pragma unroll
          for (int i = 0; i < 8; ++i) ok = ok && (unsigned)(g[j][i] >> 32) == tag;
          const float gt = bfr((u2f((unsigned)g[j][0]) + u2f((unsigned)g[j][4])) + (u2f((unsigned)g[j][2]) + u2f((unsigned)g[j][6])));
          const float up = bfr((u2f((unsigned)g[j][1]) + u2f((unsigned)g[j][5])) + (u2f((unsigned)g[j][3]) + u2f((unsigned)g[j][7])));
          hv[j] = bfr(bfr(gt * sigmoid_f(gt)) * up);
        }
        if (all_true(ok) || spins > SPIN_CAP) break;
        __builtin_amdgcn_s_sleep(16);
      }
      float hs[14];
#pragma unroll
      for (int i = 0; i < 14; ++i) hs[i] = shfl_f(hv[i / 7], 8 * (i % 7) + (lane >> 3));
      float a[8];
#pragma unroll
      for (int e = 0; e < 8; ++e) a[e] = 0.f;
#pragma unroll
      for (int i = 0; i < 14; ++i)
#pragma unroll
        for (int e = 0; e < 4; ++e) { a[2 * e] += hs[i] * blo(wd[i][e]); a[2 * e + 1] += hs[i] * bhi(wd[i][e]); }
#pragma unroll
      for (int e = 0; e < 8; ++e) a[e] = sum_hi3(a[e]);
      if (lane < 8) {
        *(f32x4*)(red + 128 + pa * 64 + lane * 8) = (f32x4){a[0], a[1], a[2], a[3]};
        *(f32x4*)(red + 128 + pa * 64 + lane * 8 + 4) = (f32x4){a[4], a[5], a[6], a[7]};
      }
      CFENCE();
      if (pa == 1) { if (lane == 0) misc[C_DN] = 1u; }
      else {
        (void)wait_ge<1>(misc + C_DN, 1u);
        const float y = red[128 + lane] + red[192 + lane];
        __hip_atomic_store((gu64*)(ws + OFF_YG) + fg * 1024 + cg * 64 + lane, ((u64)tag << 32) | f2u(y), RLX_AGENT);
      }
    }
    if (pa == 1 && (int)(idx >> 7) == mt3) {
      const float xe = u2f((unsigned)x[(size_t)idx * DM + 64 * nt3 + lane] << 16);
      const unsigned ob = bestk >> 16;
      const float rsel = bfr(sigmoid_f(u2f(((ob & 0x8000u) ? (ob & 0x7FFFu) : (~ob & 0xFFFFu)) << 16)));
      float y = 0.f, gt = 0.f;
      __builtin_amdgcn_s_sleep(30);
#pragma unroll 1
      for (unsigned spins = 0;; ++spins) {
        u64 g[17];
#pragma unroll
        for (int i = 0; i < 16; ++i) g[i] = __hip_atomic_load((gu64*)(ws + OFF_YG) + i * 1024 + 64 * nt3 + lane, RLX_AGENT);
        g[16] = __hip_atomic_load((gu64*)(ws + OFF_GG) + idx, RLX_AGENT);
        bool ok = (unsigned)(g[16] >> 32) == tag;
        gt = u2f((unsigned)g[16]); y = 0.f;
#pragma unroll
        for (int i = 0; i < 16; ++i) { ok = ok && (unsigned)(g[i] >> 32) == tag; y += u2f((unsigned)g[i]); }
        if (all_true(ok) || spins > SPIN_CAP) break;
        __builtin_amdgcn_s_sleep(8);
      }
      red[256 + lane] = bfr(bfr(1.0f - gt) * bfr(xe + bfr(bfr(y) * rsel))) - bfr(bfr(1.0f - gt) * xe);
      CFENCE();
      if (lane == 0) misc[C_Y] = 1u;
    }
    return;
  }

  if (w == 0) misc[lane] = 0u;
  const int hh = w & 1, wr = w >> 1;
  const int c16 = lane & 15, g = lane >> 4;
  const int fsw = (c16 >> 3) | ((g & 1) << 1);
  const int head = 2 * nt + hh;
  int aoff[2], boff[2];
#pragma unroll
  for (int i = 0; i < 2; ++i) {
    aoff[i] = (wr * 64 + c16) * 128 + (((4 * i + g) ^ (c16 >> 1)) * 16);
    boff[i] = 16384 + (8 * g + (c16 >> 2)) * 128 + (((2 * hh + (i ^ wr)) ^ fsw) * 32) + (c16 & 3) * 8;
  }
  f32x4 acc[3][4][2];
#pragma unroll
  for (int i = 0; i < 24; ++i) { const float s0 = i < 16 ? 1.f : 0.f; acc[i / 8][(i >> 1) & 3][i & 1] = (f32x4){s0, s0, s0, s0}; PIN(acc[i / 8][(i >> 1) & 3][i & 1]); }
  bf16x8 a[4][4], b[2][6], xb[2];
  s16x4 tlo;
#define A_AT(sl, h, mi) (*(const bf16x8*)(smem + (sl) * SLOT + aoff[h] + (mi) * 2048))
#define B_AT(sl, h, s, n) (smem + (sl) * SLOT + boff[n] + (s) * 8192 + (h) * 4096)
#define MM(s, mi, n, A, B) do { if ((s) == 0) AMFMA(acc[0][mi][n], B, A); else AMFMA(acc[s][mi][n], A, B); } while (0)
#define RD(sl, h, u)                                                                                       \
  do {                                                                                                     \
    if ((u) == 0) a[h][0] = A_AT(sl, h, 0);                                                                \
    else if ((u) >= 13) a[h][(u) - 12] = A_AT(sl, h, (u) - 12);                                            \
    else {                                                                                                 \
      const int j_ = (((u) - 1) / 2 + 2) % 6;                                                              \
      if ((u) & 1) tlo = TRD(B_AT(sl, h, j_ >> 1, j_ & 1));                                                \
      else b[h][j_] = CAT(tlo, TRD(B_AT(sl, h, j_ >> 1, j_ & 1) + 512));                                   \
    }                                                                                                      \
  } while (0)
#define STEP(h, rs, LATE)                                                                                  \
  _Pragma("unroll") for (int n_ = 0; n_ < 24; ++n_) {                                                      \
    const int jj_ = (n_ % 6 + 2) % 6;                                                                      \
    MM(jj_ >> 1, n_ / 6, jj_ & 1, a[h][n_ / 6], b[h][jj_]); SB();                                          \
    if (n_ < 16) { RD(rs, 1 - (h), n_); } else { LATE; }                                                   \
    SB();                                                                                                  \
  }
#define LGKM0() __builtin_amdgcn_s_waitcnt(0xC07F)
#define STAGE(sl, nx, LATE, END) do { STEP(0, sl, (void)0); LGKM0(); BAR(); STEP(1, nx, LATE); END; SB(); } while (0)

  WAIT_LGKM0();
  BAR();
#pragma unroll
  for (int u = 0; u < 16; ++u) RD(0, 0, u);
  LGKM0();
  SB();
#pragma unroll 1
  for (int it = 0; it < 4; ++it) { STAGE(0, 1, (void)0, LGKM0()); STAGE(1, 2, (void)0, LGKM0()); STAGE(2, 0, (void)0, LGKM0()); }
  STAGE(0, 1, (void)0, LGKM0());
#define TA(u) ((u) == 0 ? 0 : (u) == 1 ? 2 : (u) == 2 ? 1 : 3)
#define T_SL(u) ((u) < 2 ? 2 : 0)
  STAGE(1, 2, { if (n_ < 20) a[2][n_ - 16] = A_AT(2, 1, n_ - 16);
                else if ((n_ & 1) == 0) tlo = TRD(B_AT(2, 1, 1, (n_ - 20) >> 1));
                else xb[(n_ - 20) >> 1] = CAT(tlo, TRD(B_AT(2, 1, 1, (n_ - 20) >> 1) + 512)); }, (void)0);
#define P_S(e) ((e) < 4 ? 1 : (e) < 10 ? 2 : 0)
#define P_U(e) ((e) < 4 ? 2 + (e) / 2 : 1 + (((e) - 4) % 6) / 2)
#define POOL(e) ((e) % 10 < 6 ? b[1][(e) % 10] : (e) % 10 < 8 ? b[0][(e) % 10 - 4] : xb[(e) % 10 - 8])
#define P_RD(e, hi)                                                                                        \
  do {                                                                                                     \
    const char* p_ = B_AT(T_SL(P_U(e)), P_U(e) & 1, P_S(e), (e) & 1);                                      \
    if (!(hi)) tlo = TRD(p_);                                                                              \
    else if ((e) % 10 < 6) b[1][(e) % 10] = CAT(tlo, TRD(p_ + 512));                                       \
    else if ((e) % 10 < 8) b[0][(e) % 10 - 4] = CAT(tlo, TRD(p_ + 512));                                   \
    else xb[(e) % 10 - 8] = CAT(tlo, TRD(p_ + 512));                                                       \
  } while (0)
#define T_B(s, u, n) ((u) == 0 ? b[0][2 * (s) + (n)] : ((s) == 1 && (u) == 1) ? xb[n] : POOL((s) == 1 ? 2 * (u) - 4 + (n) : ((s) == 2 ? 4 : 10) + 2 * ((u) - 1) + (n)))
#define T_MM(s, j) MM(s, (j) & 3, ((j) >> 2) & 1, a[TA((j) >> 3)][(j) & 3], T_B(s, (j) >> 3, ((j) >> 2) & 1))
  unsigned kp[16], kmin = 0xFFFFFFFFu, ltag;
  {
    const unsigned fl = min(misc[C_F15], misc[C_F15 + 1]);
    ltag = misc[C_TAG];
    SB();
#pragma unroll
    for (int j = 0; j < 32; ++j) {
      T_MM(1, j); SB();
      if (j == 3) { if (__builtin_expect(rfl(fl) == 0u, 0)) { (void)wait_ge<1>(misc + C_F15, 1u); (void)wait_ge<1>(misc + C_F15 + 1, 1u); } CFENCE(); }
      if (j >= 3 && j < 19) {
        const int u = 2 + ((j - 3) >> 3), r = (j - 3) & 7;
        if (r == 0 || (r >= 3 && r < 6)) a[TA(u)][r ? r - 2 : 0] = A_AT(0, u & 1, r ? r - 2 : 0);
        else if (r < 3) P_RD(2 * (u - 2), r - 1);
        else P_RD(2 * (u - 2) + 1, r - 6);
      }
      if (j >= 20) P_RD(4 + ((j - 20) >> 1), j & 1);
      SB();
    }
    float e0, e1;
#pragma unroll
    for (int j = 0; j < 32; ++j) {
      T_MM(2, j); SB();
      const int i = j >> 1;
      const float u0 = acc[1][(i >> 1) & 3][i >> 3][2 * (i & 1)], u1 = acc[1][(i >> 1) & 3][i >> 3][2 * (i & 1) + 1];
      if ((j & 1) == 0) { e0 = phi_e(u0); e1 = phi_e(u1); }
      else { kp[i] = cvt_pk(fmax_raw(u0, e0), fmax_raw(u1, e1)); kmin = pkmin(kmin, kp[i]); }
      if (j < 8) P_RD(10 + (j >> 1), j & 1);
      if (j >= 16 && j < 20) P_RD(14 + ((j - 16) >> 1), j & 1);
      SB();
    }
  }
  NOP8(acc[2]);
  const unsigned tag = rfl(ltag);
  const __amdgpu_buffer_rsrc_t kvr = MKRSRC(ws + OFF_KVP, NH * 2 * 16 * 1024), zr = MKRSRC(ws + OFF_ZP, NH * 2 * 16 * 128);
  const f32x4 zero = {0.f, 0.f, 0.f, 0.f};
#define TAKE(flag, READS)                                                                                  \
  _Pragma("unroll 1") for (unsigned spins = 0; spins < (1u << 22); ++spins) {                              \
    const unsigned f_ = misc[flag];                                                                        \
    CFENCE();                                                                                              \
    READS;                                                                                                 \
    if (__builtin_expect(rfl(f_) != 0u, 1)) break;                                                         \
  }
  {
    phi_fix(kp, kmin);
    const u32x4 ones = {0x3F803F80u, 0x3F803F80u, 0x3F803F80u, 0x3F803F80u};
#define KF(j, n) FRAG(((u32x4){kp[8 * (n) + 4 * (j)], kp[8 * (n) + 4 * (j) + 1], kp[8 * (n) + 4 * (j) + 2], kp[8 * (n) + 4 * (j) + 3]}))
#define VF(j, n) FRAG(((u32x4){cvt_pk(acc[2][2 * (j)][n][0], acc[2][2 * (j)][n][1]), cvt_pk(acc[2][2 * (j)][n][2], acc[2][2 * (j)][n][3]), \
                               cvt_pk(acc[2][2 * (j) + 1][n][0], acc[2][2 * (j) + 1][n][1]), cvt_pk(acc[2][2 * (j) + 1][n][2], acc[2][2 * (j) + 1][n][3])}))
    char* xs = smem + L_X1 + w * 2112;
    const char* xo = smem + L_X1 + (w ^ 2) * 2112;
    f32x4 kv[2], z;
    {
      const f32x4 zs = MFMA(KF(1, 1), FRAG(ones), MFMA(KF(0, 1), FRAG(ones), zero));
      const bf16x8 v0 = VF(0, 1), v1 = VF(1, 1);
      const f32x4 s1 = MFMA(KF(1, 1), v1, MFMA(KF(0, 1), v0, zero)), s0 = MFMA(KF(1, 0), v1, MFMA(KF(0, 0), v0, zero));
      const bf16x8 u0 = VF(0, 0), u1 = VF(1, 0);
      if (c16 == 0) *(f32x4*)(xs + 2048 + g * 16) = zs;
      *(f32x4*)(xs + lane * 16) = s1;
      *(f32x4*)(xs + 1024 + lane * 16) = s0;
      CFENCE();
      if (lane == 0) misc[C_X1 + w] = 1u;
      SB();
      kv[0] = MFMA(KF(1, 0), u1, MFMA(KF(0, 0), u0, zero)); kv[1] = MFMA(KF(1, 1), u1, MFMA(KF(0, 1), u0, zero));
      z = MFMA(KF(1, 0), FRAG(ones), MFMA(KF(0, 0), FRAG(ones), zero));
    }
    f32x4 p0, p1, pz;
    TAKE(C_X1 + (w ^ 2), { p0 = *(volatile lf32x4*)(xo + lane * 16); p1 = *(volatile lf32x4*)(xo + 1024 + lane * 16); pz = *(volatile lf32x4*)(xo + 2048 + g * 16); });
    const f32x4 m0_ = kv[0] + p0, m1_ = kv[1] + p1;
    float am = fmaxf(fabsf(m0_[0]), fabsf(m1_[0]));
#pragma unroll
    for (int e = 1; e < 4; ++e) am = fmaxf(am, fmaxf(fabsf(m0_[e]), fabsf(m1_[e])));
    unsigned ex = 141u;
    if (__builtin_expect(!all_true(am < 32768.f) || all_true(am < 0.25f), 0)) ex = max(wave_max(f2u(am)) >> 23, 15u);
    const float inv = u2f((268u - ex) << 23);
#define PKH(a_, b_) __builtin_bit_cast(unsigned, __builtin_convertvector((f32x2){(a_) * inv, (b_) * inv}, f16x2_t))
    const u32x4 hv = {PKH(m0_[0], m0_[1]), PKH(m0_[2], m0_[3]), PKH(m1_[0], m1_[1]), PKH(m1_[2], m1_[3])};
    const unsigned rec = (unsigned)((head * 2 + wr) * 16 + mt);
    __builtin_amdgcn_raw_buffer_store_b128(hv, kvr, rec * 1024 + lane * 16, 0, 16);
    u32x4 zv = __builtin_bit_cast(u32x4, z + pz);
    if (lane == 1) { zv[0] = (ex - 14u) << 23; zv[1] = tag; }
    if (c16 == 0 || lane == 1) __builtin_amdgcn_raw_buffer_store_b128(zv, zr, rec * 128 + g * 16 + c16 * 64, 0, 16);
  }
  const unsigned lidx = misc[C_IDX];
  SB();
#pragma unroll
  for (int j = 0; j < 32; ++j) { T_MM(0, j); SB(); }
  NOP8(acc[0]);
  unsigned pq[16], qmin = 0xFFFFFFFFu;
#define PHIQ(j) do { pq[j] = phi2a(acc[0][(j) >> 2][((j) >> 1) & 1][2 * ((j) & 1)], acc[0][(j) >> 2][((j) >> 1) & 1][2 * ((j) & 1) + 1]); qmin = pkmin(qmin, pq[j]); } while (0)
  const int tok = m0 + 32 * w + c16;
  const size_t eoff = (size_t)tok * DM + 64 * nt3 + 16 * (g & 1) + 8 * (g >> 1);
  u32x4 xv[4];
  u64 gg[4];
  const gu64* gp = (const gu64*)(ws + OFF_GG) + m0 + 64 * wr + c16;
#pragma unroll
  for (int i = 0; i < 4; ++i) gg[i] = __hip_atomic_load(gp + 16 * i, RLX_AGENT);
#pragma unroll
  for (int i = 0; i < 4; ++i) xv[i] = *(const u32x4*)(x + eoff + (i >> 1) * (16 * DM) + (i & 1) * 32);
  const unsigned r0 = (unsigned)((head * 2 + wr) * 16);
  unsigned zo0 = (r0 + c16) * 128 + 64, zo1 = (r0 + (lane >> 2)) * 128 + (lane & 3) * 16, ko = r0 * 1024 + lane * 16;
  unsigned oo = (unsigned)(((mt * 8 + wr * 4) * NH + head) * 1024 + lane * 16);
  PIN(zo0); PIN(zo1); PIN(ko); PIN(oo);
  const unsigned one = (c16 >> 2) == g ? (0x3C00u << (16 * (c16 & 1))) : 0u;
  u32x4 se0 = {(c16 & 2) ? 0u : one, (c16 & 2) ? one : 0u, 0u, 0u}, se1 = {0u, 0u, se0[0], se0[1]};
  PIN(se0); PIN(se1);
  const f16x8_t sel0 = __builtin_bit_cast(f16x8_t, se0), sel1 = __builtin_bit_cast(f16x8_t, se1);
  const __amdgpu_buffer_rsrc_t ofr = MKRSRC(ws + OFF_OF, SEQ * DM * 2);
  u32x4 zw;
  u32x2 st;
#define Z_LOAD() do { zw = __builtin_amdgcn_raw_buffer_load_b128(zr, zo1, 0, 16); st = __builtin_amdgcn_raw_buffer_load_b64(zr, zo0, 0, 16); } while (0)
#define Z_OK() all_true(st[1] == tag && st[0] != ~0u && max(max(zw[0], zw[1]), max(zw[2], zw[3])) != ~0u)
  if (__builtin_expect(rfl(lidx) == 0u, 0)) (void)wait_ge<1>(misc + C_IDX, 1u);
  CFENCE();
#pragma unroll
  for (int j = 0; j < 16; ++j) PHIQ(j);
#pragma unroll
  for (int j = 0; j < 16; ++j) PIN(pq[j]);
  __builtin_amdgcn_s_sleep(8);
  SB(); Z_LOAD(); SB();
  phi_fix(pq, qmin);
#define GG_OK() all_true((unsigned)(gg[0] >> 32) == tag && (unsigned)(gg[1] >> 32) == tag && (unsigned)(gg[2] >> 32) == tag && (unsigned)(gg[3] >> 32) == tag)
  if (__builtin_expect(!GG_OK(), 0)) {
#pragma unroll 1
    for (unsigned spins = 0; spins <= SPIN_CAP; ++spins) {
      __builtin_amdgcn_s_sleep(4);
#pragma unroll
      for (int i = 0; i < 4; ++i) gg[i] = __hip_atomic_load(gp + 16 * i, RLX_AGENT);
      if (GG_OK()) break;
    }
  }
  float gq0 = u2f((unsigned)gg[0]), gq1 = u2f((unsigned)gg[1]), gq2 = u2f((unsigned)gg[2]), gq3 = u2f((unsigned)gg[3]);
  PIN(gq0); PIN(gq1); PIN(gq2); PIN(gq3);
  {
    bool ok = Z_OK();
    WAIT_VM(0);
    if (lane == 0) {
      const unsigned old = __hip_atomic_fetch_add((lu32*)(smem + L_CTL) + C_KARR, 1u, RLX_WG);
      if (old == 3u) __hip_atomic_store((gu32*)(ws + OFF_KVFLAG + (nt * 16 + mt) * 128), tag, RLX_AGENT);
    }
#pragma unroll 1
    for (unsigned spins = 0; !ok && spins <= SPIN_CAP; ++spins) {
      CFENCE();
      Z_LOAD();
      ok = Z_OK();
      if (!ok) __builtin_amdgcn_s_sleep(2);
    }
    CFENCE();
    f32x4 zt = __builtin_bit_cast(f32x4, zw);
    const unsigned sc = st[0];
    u32x4 t[16];
#define T_LOAD(i0, i1) _Pragma("unroll") for (int i = (i0); i < (i1); ++i) t[i] = __builtin_amdgcn_raw_buffer_load_b128(kvr, ko, i * 1024, 16)
#define Z_RED(e) do { zt[e] += dppf<0x124>(zt[e]); zt[e] = sum_hi3(zt[e]); } while (0)
#define PF(mi) FRAG(((u32x4){pq[4 * (mi)], pq[4 * (mi) + 1], pq[4 * (mi) + 2], pq[4 * (mi) + 3]}))
    char* xs = smem + L_X2 + w * 1056;
    const char* xo = smem + L_X2 + (w ^ 2) * 1056;
    T_LOAD(0, 5); SB();
    Z_RED(0); Z_RED(1); PIN(zt[0]); PIN(zt[1]); SB();
    T_LOAD(5, 8); SB();
    Z_RED(2); Z_RED(3);
    if (lane < 4) *(u32x2*)(xs + 1024 + lane * 8) = (u32x2){cvt_pk(zt[0], zt[1]), cvt_pk(zt[2], zt[3])};
    CFENCE();
    if (lane == 0) misc[C_XZ + w] = 1u;
    SB();
    T_LOAD(8, 16);
    if (lane == 0) misc[C_AG + w] = 1u;
    SB();
    f32x4 s0 = zero, s1 = zero;
#define KV_MM(i0, i1)                                                                                      \
  _Pragma("unroll") for (int i = (i0); i < (i1); ++i) {                                                    \
    s0 = __builtin_amdgcn_mfma_f32_16x16x32_f16(sel0, __builtin_bit_cast(f16x8_t, t[i]), s0, 0, 0, 0);     \
    s1 = __builtin_amdgcn_mfma_f32_16x16x32_f16(sel1, __builtin_bit_cast(f16x8_t, t[i]), s1, 0, 0, 0);     \
  }
    KV_MM(0, AGZ);
    SB();
    float rc[4];
    {
      u32x2 z0, z1;
      TAKE(C_XZ + (w ^ 2), { z0 = *(volatile lu32x2*)(xs + 1024 + g * 8); z1 = *(volatile lu32x2*)(xo + 1024 + g * 8); });
      const u32x4 zf = {z0[0], z0[1], z1[0], z1[1]};
#pragma unroll
      for (int mi = 0; mi < 4; ++mi) {
        const f32x4 d = MFMA(FRAG(zf), PF(mi), zero);
        rc[mi] = (mi == 0 ? gq0 : mi == 1 ? gq1 : mi == 2 ? gq2 : gq3) * __builtin_amdgcn_rcpf(bfr(bfr(d[0]) + 1e-6f));
        PIN(rc[mi]);
      }
    }
    SB();
    KV_MM(AGZ, 16);
    float sum[8] = {s0[0], s0[1], s0[2], s0[3], s1[0], s1[1], s1[2], s1[3]};
    if (__builtin_expect(!all_true(sc == 0x3F800000u && !__builtin_isunordered(s0[0], s1[0])), 0)) {
      gu32* flags = (gu32*)(ws + OFF_KVFLAG + (nt * 16 + c16) * 128);
#pragma unroll 1
      for (unsigned spins = 0; spins <= SPIN_CAP; ++spins) {
        if (all_true(__hip_atomic_load(flags, RLX_AGENT) == tag)) break;
        __builtin_amdgcn_s_sleep(2);
      }
      CFENCE();
      T_LOAD(0, 16);
      if (all_true(sc == 0x3F800000u)) {
        s0 = zero; s1 = zero;
        KV_MM(0, 16);
#pragma unroll
        for (int e = 0; e < 4; ++e) { sum[e] = s0[e]; sum[4 + e] = s1[e]; }
      } else {
#pragma unroll
        for (int e = 0; e < 8; ++e) sum[e] = 0.f;
#pragma unroll
        for (int i = 0; i < 16; ++i) {
          const float s_ = u2f((unsigned)__builtin_amdgcn_readlane((int)sc, i));
#pragma unroll
          for (int e = 0; e < 4; ++e) {
            const unsigned tw = t[i][e];
            const f16x2_t h = __builtin_bit_cast(f16x2_t, tw);
            sum[2 * e] = __builtin_fmaf((float)h[0], s_, sum[2 * e]); sum[2 * e + 1] = __builtin_fmaf((float)h[1], s_, sum[2 * e + 1]);
          }
        }
      }
    }
    const u32x4 kown = {cvt_pk(sum[0], sum[1]), cvt_pk(sum[2], sum[3]), cvt_pk(sum[4], sum[5]), cvt_pk(sum[6], sum[7])};
    *(u32x4*)(xs + lane * 16) = (u32x4){kown[2], kown[3], kown[0], kown[1]};
    CFENCE();
    if (lane == 0) misc[C_X2 + w] = 1u;
    SB();
    u32x2 ow[4];
#pragma unroll
    for (int mi = 0; mi < 4; ++mi) {
      const f32x4 o = MFMA(FRAG(kown), PF(mi), zero);
      ow[mi] = (u32x2){cvt_pk(o[0] * rc[mi], o[1] * rc[mi]), cvt_pk(o[2] * rc[mi], o[3] * rc[mi])};
      PIN(ow[mi]);
    }
    u32x4 koth;
    TAKE(C_X2 + (w ^ 2), koth = *(volatile lu32x4*)(xo + lane * 16));
#pragma unroll
    for (int mi = 0; mi < 4; ++mi) {
      const f32x4 o = MFMA(FRAG(koth), PF(mi), zero);
      const u32x4 v = {ow[mi][0], ow[mi][1], cvt_pk(o[0] * rc[mi], o[1] * rc[mi]), cvt_pk(o[2] * rc[mi], o[3] * rc[mi])};
      __builtin_amdgcn_raw_buffer_store_b128(v, ofr, oo, mi * (NH * 1024), 16);
    }
  }
  SB();
  f32x4 oc[4][2];
#pragma unroll
  for (int i = 0; i < 4; ++i) {
    const int tt = i >> 1, n = 2 * (i & 1);
    const float omg = bfr(1.0f - (tt ? (hh ? gq3 : gq1) : (hh ? gq2 : gq0)));
#pragma unroll
    for (int k = 0; k < 2; ++k) {
      const auto sw = __builtin_amdgcn_permlane16_swap(cvt_pk(omg * blo(xv[i][k]), omg * bhi(xv[i][k])),
                                                       cvt_pk(omg * blo(xv[i][2 + k]), omg * bhi(xv[i][2 + k])), false, false);
      oc[n][tt][2 * k] = blo(sw[0]); oc[n][tt][2 * k + 1] = bhi(sw[0]);
      oc[n + 1][tt][2 * k] = blo(sw[1]); oc[n + 1][tt][2 * k + 1] = bhi(sw[1]);
    }
    PIN(oc[n][tt]); PIN(oc[n + 1][tt]);
  }
  int wof[2][2][4];
#pragma unroll
  for (int n = 0; n < 16; ++n) {
    wof[n >> 3][(n >> 2) & 1][n & 3] = 16384 + ((n >> 2) & 1) * 65536 + (((n >> 3) ^ wr) * 16 + 4 * g + (c16 >> 2)) * 128 + (((n & 3) ^ fsw) * 32) + (c16 & 3) * 8;
    PIN(wof[n >> 3][(n >> 2) & 1][n & 3]);
  }
  const unsigned obase = (unsigned)(((mt3 * 8 + 2 * w) * NH) * 1024 + lane * 16);
  bf16x8 wf[2][4];
#define W_AT(hd, n, r) (smem + wof[r][WOPOS((hd) >> 1) >> 3][n] + (WOPOS((hd) >> 1) & 7) * 8192 + ((hd) & 1) * 4096)
  const unsigned lw = min(misc[C_WO], misc[C_WO + 1]);
  WAIT_VM(0);
  if (lane == 0) {
    const unsigned old = __hip_atomic_fetch_add((lu32*)(smem + L_CTL) + C_OARR, 1u, RLX_WG);
    if (old == 3u) {
      __hip_atomic_store((gu32*)(ws + OFF_OFLAG + (mt * 16 + nt) * 128), tag, RLX_AGENT);
      misc[C_T6] = (unsigned)__builtin_amdgcn_s_memrealtime() | 1u;
    }
  }
  CFENCE();

  if (__builtin_expect(rfl(lw) == 0u, 0)) { (void)wait_ge<1>(misc + C_WO, 1u); (void)wait_ge<1>(misc + C_WO + 1, 1u); }
  CFENCE();
#pragma unroll
  for (int n = 0; n < 4; ++n) wf[0][n] = CAT(TRD(W_AT(0, n, 0)), TRD(W_AT(0, n, 1)));
  u32x4 of[OPF + 1][4];
  unsigned rdy = 0;
#define O_READY(c) do { if (__builtin_expect(rdy <= (unsigned)(c), 0)) rdy = wait_ge<1>(misc + C_ORDY, (unsigned)(c) + 1u); CFENCE(); } while (0)
#define O_LD1(c, k4) of[(c) % (OPF + 1)][k4] = __builtin_amdgcn_raw_buffer_load_b128(ofr, obase + ((k4) >> 1) * 1024 + ((k4) & 1) * (NH * 1024), ((rot + (c)) & 15) * 2048, 16)
#pragma unroll
  for (int c = 0; c < OPF; ++c) { O_READY(c); O_LD1(c, 0); O_LD1(c, 1); O_LD1(c, 2); O_LD1(c, 3); }
  unsigned li = 0;
#define P3_MM(c, j)                                                                                        \
  do {                                                                                                     \
    constexpr int hd = 2 * (c) + ((j) >> 3), n = ((j) >> 1) & 3;                                           \
    AMFMA(oc[n][(j) & 1], wf[(j) >> 3][n], of[(c) % (OPF + 1)][2 * ((j) >> 3) + ((j) & 1)]); SB();         \
    if (hd + 1 < 32) {                                                                                     \
      if (((j) & 1) == 0) tlo = TRD(W_AT((hd + 1) & 31, n, 0)); else wf[1 - ((j) >> 3)][n] = CAT(tlo, TRD(W_AT((hd + 1) & 31, n, 1))); \
    }                                                                                                      \
    if ((c) + OPF < 16) {                                                                                  \
      if ((j) == 0) O_READY((c) + OPF);                                                                    \
      if (((j) & 3) == 1) O_LD1((c) + OPF, (j) >> 2);                                                      \
    }                                                                                                      \
    if ((c) == 15 && (j) == 8) li = misc[C_IDX];                                                           \
    SB();                                                                                                  \
  } while (0)
#define P3_4(c, j) P3_MM(c, j); P3_MM(c, (j) + 1); P3_MM(c, (j) + 2); P3_MM(c, (j) + 3)
#define P3_C(c) P3_4(c, 0); P3_4(c, 4); P3_4(c, 8); P3_4(c, 12)
  P3_C(0); P3_C(1); P3_C(2); P3_C(3); P3_C(4); P3_C(5); P3_C(6); P3_C(7);
  P3_C(8); P3_C(9); P3_C(10); P3_C(11); P3_C(12); P3_C(13); P3_C(14); P3_C(15);
  NOP8(oc);
  li = rfl(li);
  if (__builtin_expect(li == 0u, 0)) li = wait_ge<1>(misc + C_IDX, 1u);
  const int idx = (int)li - 1;
  if ((idx >> 5) == mt3 * 4 + w) {
    f32x4 dl[4];
    TAKE(C_Y, { _Pragma("unroll") for (int n = 0; n < 4; ++n) dl[n] = *(volatile lf32x4*)(smem + L_RED + 1024 + n * 64 + g * 16); });
    const bool mine = c16 == (idx & 15);
    if (idx & 16) {
#pragma unroll
      for (int n = 0; n < 4; ++n) oc[n][1] += mine ? dl[n] : zero;
    } else {
#pragma unroll
      for (int n = 0; n < 4; ++n) oc[n][0] += mine ? dl[n] : zero;
    }
  }
#pragma unroll
  for (int i = 0; i < 4; ++i) {
    const int tt = i >> 1, n = 2 * (i & 1);
    u32x4 o;
#pragma unroll
    for (int k = 0; k < 2; ++k) {
      const auto sw = __builtin_amdgcn_permlane16_swap(cvt_pk(oc[n][tt][2 * k], oc[n][tt][2 * k + 1]),
                                                       cvt_pk(oc[n + 1][tt][2 * k], oc[n + 1][tt][2 * k + 1]), false, false);
      o[k] = sw[0]; o[2 + k] = sw[1];
    }
    __builtin_amdgcn_raw_buffer_store_b128(o, MKRSRC(out, SEQ * DM * 2), (unsigned)(eoff * 2) + tt * (32 * DM) + (i & 1) * 64, 0, 17);
  }
}

extern "C" void kernel_launch(void* const* d_in, const int* in_sizes, int n_in,
                              void* d_out, int out_size, void* d_ws, size_t ws_size,
                              hipStream_t stream) {
  char* ws = (char*)d_ws;
  (void)hipFuncSetAttribute((const void*)fused_kernel, hipFuncAttributeMaxDynamicSharedMemorySize, L_TOTAL);
  fused_kernel<<<256, 512, L_TOTAL, stream>>>((const u16*)d_in[0], (const u16*)d_in[1], (const u16*)d_in[2], ws, (u16*)d_out,
                                              (const u16*)d_in[4], (const u16*)d_in[8], (const u16*)d_in[5], (const u16*)d_in[6],
                                              (const u16*)d_in[7]);
}
```
